```python
import torch

def _softmax(x, axis=-1):
    return torch.softmax(x, dim=axis)
def _gelu(x):
    return torch.nn.functional.gelu(x, approximate="tanh")
def _silu(x):
    return torch.nn.functional.silu(x)
def _sigmoid(x):
    return torch.sigmoid(x)
def _layernorm(x, axis=-1, eps=1e-5):
    m = x.mean(dim=axis, keepdim=True)
    v = x.var(dim=axis, keepdim=True, unbiased=False)
    return (x - m) / torch.sqrt(v + eps)
def _scatter_add(tgt, idx, src, axis=0):
    return tgt.clone().index_add_(axis, idx, src)
def _segment_sum(src, seg, n):
    out = torch.zeros((n,) + src.shape[1:], dtype=src.dtype)
    return out.index_add_(0, seg, src)
def _pad(x, pw):
    flat = tuple(v for pair in reversed(pw) for v in pair)
    return torch.nn.functional.pad(x, flat)

kernel_name = 'standalone__paged_block_table_kv_pagedattention__sampling'

def setup_inputs():

    _w = (lambda a: (lambda t: t.to(torch.bfloat16) if t.is_floating_point() else t)(torch.as_tensor(a)))
    B, Hq, Hkv, Sq, Skv, D = 16, 32, 4, 8, 4096, 128
    bs = 16
    nbps = max(1, Skv // bs)
    num_phys = B * nbps + 4
    bt = torch.randperm(num_phys)[: B * nbps].reshape(B, nbps).to(torch.int64)
    cl = (nbps * bs - (torch.arange(1, B + 1, dtype=torch.int64) * 2654435761) % max(1, nbps * bs // 2)).to(torch.int64)
    return {
        "q": _w((lambda *s: torch.randn(*s, dtype=torch.bfloat16) * 0.8)(B, Hq, Sq, D)),
        "k_cache": _w((lambda *s: torch.randn(*s, dtype=torch.bfloat16) * 0.8)(num_phys, bs, Hkv, D)),
        "v_cache": _w((lambda *s: torch.randn(*s, dtype=torch.bfloat16) * 0.05)(num_phys, bs, Hkv, D)),
        "block_table": _w(bt),
        "context_lens": _w(cl),
    }

def reference(q, k_cache, v_cache, block_table, context_lens):
    B, Hq, Sq, D = q.shape
    bs = k_cache.shape[1]
    Hkv = k_cache.shape[2]
    nbps = block_table.shape[1]
    Skv = nbps * bs
    bt = block_table.reshape(-1)
    k = (lambda x, *d: x.permute(*d))(k_cache[bt].reshape(B, Skv, Hkv, D), 0, 2, 1, 3)
    v = (lambda x, *d: x.permute(*d))(v_cache[bt].reshape(B, Skv, Hkv, D), 0, 2, 1, 3)
    g = Hq // Hkv
    qg = q.reshape(B, Hkv, g, Sq, D)
    scores = torch.einsum("bhgqd,bhkd->bhgqk", qg, k) * (D ** -0.5)
    pos = torch.arange(Skv)
    msk = pos[None, :] < context_lens[:, None]
    scores = torch.where(msk[:, None, None, None, :], scores, -1e9)
    p = _softmax(scores, axis=-1)
    out = torch.einsum("bhgqk,bhkd->bhgqd", p, v)
    return out.reshape(B, Hq, Sq, D)
```

```cpp
#include <hip/hip_runtime.h>
#include <hip/hip_bf16.h>
#include <stdint.h>

namespace {
constexpr int B = 16, HQ = 32, HKV = 4, SQ = 8, D = 128, PAGE = 16, NBPS = 256;
constexpr int SKV = NBPS * PAGE;
constexpr int ROWS = (HQ / HKV) * SQ;
constexpr int NSEG = B * HKV;
constexpr int NWG = 256;
constexpr int NSLOT = 8;
constexpr int SLOT_BYTES = 16384;
constexpr int MAX_PART = NWG + NSEG;
constexpr float C2 = 0.08838834764831845f * 1.4426950408889634f;

using bf16x8 = __attribute__((ext_vector_type(8))) short;
using s16x4 = __attribute__((ext_vector_type(4))) short;
using f32x4 = __attribute__((ext_vector_type(4))) float;
typedef short v4i16_t __attribute__((ext_vector_type(4)));
typedef __attribute__((address_space(3))) const char* lds_cptr;

__device__ __forceinline__ void dma16(unsigned voff, const void* sbase, unsigned lds_dst) {
  unsigned keep;
  asm volatile("s_mov_b32 %0, m0\n\ts_mov_b32 m0, %3\n\ts_nop 0\n\tglobal_load_lds_dwordx4 %1, %2\n\ts_mov_b32 m0, %0"
               : "=&s"(keep) : "v"(voff), "s"(sbase), "s"(lds_dst) : "memory");
}
__device__ __forceinline__ s16x4 vtr(lds_cptr p) {
  return __builtin_bit_cast(s16x4, __builtin_amdgcn_ds_read_tr16_b64_v4i16((__attribute__((address_space(3))) v4i16_t*)p));
}
__device__ __forceinline__ unsigned cvtpk(float lo, float hi) {
  unsigned r; asm("v_cvt_pk_bf16_f32 %0, %1, %2" : "=v"(r) : "v"(lo), "v"(hi)); return r;
}
__device__ __forceinline__ float xrow_max(float x) {
  auto s = __builtin_amdgcn_permlane16_swap(__float_as_uint(x), __float_as_uint(x), false, false);
  x = fmaxf(__uint_as_float(s[0]), __uint_as_float(s[1]));
  auto t = __builtin_amdgcn_permlane32_swap(__float_as_uint(x), __float_as_uint(x), false, false);
  return fmaxf(__uint_as_float(t[0]), __uint_as_float(t[1]));
}
__device__ __forceinline__ float xrow_sum(float x) {
  auto s = __builtin_amdgcn_permlane16_swap(__float_as_uint(x), __float_as_uint(x), false, false);
  x = __uint_as_float(s[0]) + __uint_as_float(s[1]);
  auto t = __builtin_amdgcn_permlane32_swap(__float_as_uint(x), __float_as_uint(x), false, false);
  return __uint_as_float(t[0]) + __uint_as_float(t[1]);
}

struct SeqInfo { int P[B + 1]; int c[B]; int len[B]; int uni[B]; };
__device__ __forceinline__ void seq_info(const int* __restrict__ lens, SeqInfo& s) {
  int acc = 0;
#pragma unroll
  for (int j = 0; j < B; ++j) {
    int l = lens[j]; int u = (l <= 0); l = u ? SKV : (l > SKV ? SKV : l);
    const int c = ((l + 31) >> 5) + 1;
    s.P[j] = acc; s.c[j] = c; s.len[j] = l; s.uni[j] = u; acc += c * HKV;
  }
  s.P[B] = acc;
}
}

__global__ void __launch_bounds__(256, 1)
attn_split(const __hip_bfloat16* __restrict__ q, const __hip_bfloat16* __restrict__ kc, const __hip_bfloat16* __restrict__ vc,
           const int* __restrict__ bt, const int* __restrict__ lens, float* __restrict__ po, float* __restrict__ ml) {
  __shared__ __attribute__((aligned(16))) char lds[NSLOT * SLOT_BYTES];
  const int tid = threadIdx.x, lane = tid & 63;
  const int w = __builtin_amdgcn_readfirstlane(tid >> 6);
  const int wg = blockIdx.x;
  const int g = lane >> 4, l15 = lane & 15;

  SeqInfo si; seq_info(lens, si);
  const int NI = si.P[B];
  int x0 = (wg * NI) >> 8, x1 = ((wg + 1) * NI) >> 8;
  auto locate = [&](int x, int& b_, int& kvh_, int& u_, int& len_, int& uni_) {
    int bb = 0, Pb = 0, cb = si.c[0], lb = si.len[0], ub = si.uni[0];
#pragma unroll
    for (int j = 1; j < B; ++j) { const bool ge = x >= si.P[j]; bb = ge ? j : bb; Pb = ge ? si.P[j] : Pb; cb = ge ? si.c[j] : cb; lb = ge ? si.len[j] : lb; ub = ge ? si.uni[j] : ub; }
    const int r = x - Pb; const int kk = (r >= cb) + (r >= 2 * cb) + (r >= 3 * cb);
    b_ = bb; kvh_ = kk; u_ = r - kk * cb; len_ = lb; uni_ = ub;
  };
  int b0, k0, u0, l0, n0; locate(x0, b0, k0, u0, l0, n0);
  const int pre = (u0 > 0 && x1 > x0) ? 1 : 0;
  int n_items = x1 - x0 + pre;
  int xi = x0 + lane - pre;
  int ib, ik, iu, il, in_; locate(xi < x0 ? x0 : xi, ib, ik, iu, il, in_);
  if (xi < x0) iu = 0;
  const bool in_range = lane < n_items;
  {
    const int lastu = __builtin_amdgcn_readlane(iu, n_items > 0 ? n_items - 1 : 0);
    if (n_items > 0 && lastu == 0) n_items -= 1;
  }
  int ph0 = 0, ph1 = 0;
  const int tile = iu - 1;
  if (in_range && iu > 0) { const int2 pp = *reinterpret_cast<const int2*>(bt + ib * NBPS + 2 * tile); ph0 = pp.x; ph1 = pp.y; }
  int nval = il - 32 * tile; nval = nval > 32 ? 32 : nval;
  const int info = (ib * HKV + ik) | ((iu > 0 ? nval : 0) << 8) | ((iu == 0 ? 1 : 0) << 16) | (in_ << 17);

  const int lr = lane >> 4, lp = lane & 15;
  const int r16 = 4 * w + lr;
  const unsigned voffK = lr * 1024 + ((lp ^ r16) << 4);
  const unsigned voffV = lr * 1024 + ((lp ^ ((r16 & 7) << 1)) << 4);
  const unsigned voffQ = lr * 256 + ((lp ^ r16) << 4);
  const unsigned lds0 = (unsigned)(uintptr_t)lds;

  auto issue = [&](int i) {
    const int inf = __builtin_amdgcn_readlane(info, i);
    const unsigned dst = lds0 + (i & (NSLOT - 1)) * SLOT_BYTES + w * 1024;
    const int seg = inf & 255;
    if (inf & 0x10000) {
      const char* qb = (const char*)q + (size_t)seg * (ROWS * D * 2) + w * 1024;
#pragma unroll
      for (int j = 0; j < 4; ++j) dma16(voffQ, qb + j * 4096, dst + j * 4096);
    } else {
      const int p0 = __builtin_amdgcn_readlane(ph0, i), p1 = __builtin_amdgcn_readlane(ph1, i);
      const size_t ho = (size_t)(seg & 3) * 256 + w * 4096;
      const char* k0p = (const char*)kc + (size_t)p0 * 16384 + ho;
      const char* k1p = (const char*)kc + (size_t)p1 * 16384 + ho;
      const char* v0p = (const char*)vc + (size_t)p0 * 16384 + ho;
      const char* v1p = (const char*)vc + (size_t)p1 * 16384 + ho;
      dma16(voffK, k0p, dst); dma16(voffK, k1p, dst + 4096); dma16(voffV, v0p, dst + 8192); dma16(voffV, v1p, dst + 12288);
    }
  };

  for (int i = 0; i < NSLOT - 1 && i < n_items; ++i) issue(i);

  unsigned kaddr[4], vaddr[8];
#pragma unroll
  for (int s = 0; s < 4; ++s) kaddr[s] = l15 * 256 + (((4 * s + g) ^ l15) << 4);
  {
    const int qq = l15 >> 2, pp = l15 & 3, row = 4 * g + qq;
#pragma unroll
    for (int c = 0; c < 8; ++c) vaddr[c] = 8192 + row * 256 + ((c ^ (row & 7)) << 5) + 8 * pp;
  }

  bf16x8 qf[4];
  f32x4 o[8];
  float m = -INFINITY, l = 0.f;
#pragma unroll
  for (int c = 0; c < 8; ++c) o[c] = f32x4{0.f, 0.f, 0.f, 0.f};

  for (int i = 0; i < n_items; ++i) {
    const int rem = n_items - 1 - i;
    if (rem >= 6) asm volatile("s_waitcnt vmcnt(24)" ::: "memory");
    else if (rem == 5) asm volatile("s_waitcnt vmcnt(20)" ::: "memory");
    else if (rem == 4) asm volatile("s_waitcnt vmcnt(16)" ::: "memory");
    else if (rem == 3) asm volatile("s_waitcnt vmcnt(12)" ::: "memory");
    else if (rem == 2) asm volatile("s_waitcnt vmcnt(8)" ::: "memory");
    else if (rem == 1) asm volatile("s_waitcnt vmcnt(4)" ::: "memory");
    else asm volatile("s_waitcnt vmcnt(0)" ::: "memory");
    asm volatile("s_barrier" ::: "memory");
    if (i + NSLOT - 1 < n_items) issue(i + NSLOT - 1);

    const int inf = __builtin_amdgcn_readlane(info, i);
    const lds_cptr sb = (lds_cptr)lds + (i & (NSLOT - 1)) * SLOT_BYTES;
    if (inf & 0x10000) {
#pragma unroll
      for (int s = 0; s < 4; ++s) {
        bf16x8 v = *(const __attribute__((address_space(3))) bf16x8*)(sb + w * 4096 + kaddr[s]);
        qf[s] = (inf & 0x20000) ? bf16x8{0, 0, 0, 0, 0, 0, 0, 0} : v;
      }
      m = -INFINITY; l = 0.f;
#pragma unroll
      for (int c = 0; c < 8; ++c) o[c] = f32x4{0.f, 0.f, 0.f, 0.f};
    } else {
      const int nv = (inf >> 8) & 63;
      f32x4 s0 = f32x4{0.f, 0.f, 0.f, 0.f}, s1 = f32x4{0.f, 0.f, 0.f, 0.f};
#pragma unroll
      for (int s = 0; s < 4; ++s) {
        bf16x8 ka = *(const __attribute__((address_space(3))) bf16x8*)(sb + kaddr[s]);
        bf16x8 kb = *(const __attribute__((address_space(3))) bf16x8*)(sb + 4096 + kaddr[s]);
        s0 = __builtin_amdgcn_mfma_f32_16x16x32_bf16(ka, qf[s], s0, 0, 0, 0);
        s1 = __builtin_amdgcn_mfma_f32_16x16x32_bf16(kb, qf[s], s1, 0, 0, 0);
      }
      if (nv < 32) {
#pragma unroll
        for (int j = 0; j < 4; ++j) { if (4 * g + j >= nv) s0[j] = -INFINITY; if (16 + 4 * g + j >= nv) s1[j] = -INFINITY; }
      }
      float tm = fmaxf(fmaxf(fmaxf(s0[0], s0[1]), fmaxf(s0[2], s0[3])), fmaxf(fmaxf(s1[0], s1[1]), fmaxf(s1[2], s1[3])));
      tm = xrow_max(tm) * C2;
      const float mn = fmaxf(m, tm);
      const float alpha = __builtin_amdgcn_exp2f(m - mn);
      m = mn;
      float p0[4], p1[4], ps = 0.f;
#pragma unroll
      for (int j = 0; j < 4; ++j) {
        p0[j] = __builtin_amdgcn_exp2f(__builtin_fmaf(s0[j], C2, -mn)); p1[j] = __builtin_amdgcn_exp2f(__builtin_fmaf(s1[j], C2, -mn));
        ps += p0[j] + p1[j];
      }
      l = l * alpha + ps;
      union { unsigned u[4]; bf16x8 v; } pf;
      pf.u[0] = cvtpk(p0[0], p0[1]); pf.u[1] = cvtpk(p0[2], p0[3]); pf.u[2] = cvtpk(p1[0], p1[1]); pf.u[3] = cvtpk(p1[2], p1[3]);
#pragma unroll
      for (int c = 0; c < 8; ++c) {
        const s16x4 lo = vtr(sb + vaddr[c]), hi = vtr(sb + vaddr[c] + 4096);
        const bf16x8 vf = bf16x8{lo[0], lo[1], lo[2], lo[3], hi[0], hi[1], hi[2], hi[3]};
        o[c] *= alpha;
        o[c] = __builtin_amdgcn_mfma_f32_16x16x32_bf16(vf, pf.v, o[c], 0, 0, 0);
      }
      const bool last = (i + 1 == n_items) || (__builtin_amdgcn_readlane(info, i + 1) & 0x10000);
      if (last) {
        const int slot = wg + (inf & 255);
        float* pr = po + ((size_t)slot * ROWS + 16 * w + l15) * D + 4 * g;
#pragma unroll
        for (int c = 0; c < 8; ++c) *reinterpret_cast<f32x4*>(pr + 16 * c) = o[c];
        const float lt = xrow_sum(l);
        if (g == 0) *reinterpret_cast<float2*>(ml + ((size_t)slot * ROWS + 16 * w + l15) * 2) = make_float2(m, lt);
      }
    }
  }
}

__global__ void __launch_bounds__(256)
attn_combine(const int* __restrict__ lens, const float* __restrict__ po, const float* __restrict__ ml, __hip_bfloat16* __restrict__ out) {
  SeqInfo si; seq_info(lens, si);
  const int NI = si.P[B];
  const int gid = blockIdx.x * 256 + threadIdx.x;
  const int seg = gid >> 11, row = (gid >> 5) & 63, d4 = gid & 31;
  const int b = seg >> 2, kvh = seg & 3;
  int Pb = 0, cb = 0;
#pragma unroll
  for (int j = 0; j < B; ++j) if (j == b) { Pb = si.P[j]; cb = si.c[j]; }
  const int A = Pb + kvh * cb;
  const int w0 = ((A + 2) * NWG - 1) / NI, w1 = ((A + cb) * NWG - 1) / NI;
  float M = -INFINITY;
  for (int w = w0; w <= w1; ++w) M = fmaxf(M, ml[((size_t)(w + seg) * ROWS + row) * 2]);
  float L = 0.f; f32x4 acc = f32x4{0.f, 0.f, 0.f, 0.f};
  for (int w = w0; w <= w1; ++w) {
    const float2 e = *reinterpret_cast<const float2*>(ml + ((size_t)(w + seg) * ROWS + row) * 2);
    const float wt = __builtin_amdgcn_exp2f(e.x - M);
    const f32x4 v = *reinterpret_cast<const f32x4*>(po + ((size_t)(w + seg) * ROWS + row) * D + 4 * d4);
    L += wt * e.y; acc += v * wt;
  }
  const float inv = 1.f / L;
  union { unsigned u[2]; } r;
  r.u[0] = cvtpk(acc[0] * inv, acc[1] * inv); r.u[1] = cvtpk(acc[2] * inv, acc[3] * inv);
  *reinterpret_cast<uint2*>(out + ((size_t)seg * ROWS + row) * D + 4 * d4) = make_uint2(r.u[0], r.u[1]);
}

extern "C" void kernel_launch(void* const* d_in, const int* in_sizes, int n_in, void* d_out, int out_size, void* d_ws, size_t ws_size,
                              hipStream_t stream) {
  const __hip_bfloat16* q = (const __hip_bfloat16*)d_in[0];
  const __hip_bfloat16* kc = (const __hip_bfloat16*)d_in[1];
  const __hip_bfloat16* vc = (const __hip_bfloat16*)d_in[2];
  const int* bt = (const int*)d_in[3];
  const int* lens = (const int*)d_in[4];
  float* po = (float*)d_ws;
  float* ml = po + (size_t)MAX_PART * ROWS * D;
  attn_split<<<NWG, 256, 0, stream>>>(q, kc, vc, bt, lens, po, ml);
  attn_combine<<<NSEG * ROWS * 32 / 256, 256, 0, stream>>>(lens, po, ml, (__hip_bfloat16*)d_out);
}
```

```cpp
#include <hip/hip_runtime.h>
#include <hip/hip_bf16.h>
#include <stdint.h>

namespace {
constexpr int B = 16, HQ = 32, HKV = 4, SQ = 8, D = 128, PAGE = 16, NBPS = 256;
constexpr int SKV = NBPS * PAGE;
constexpr int ROWS = (HQ / HKV) * SQ;
constexpr int NSEG = B * HKV;
constexpr int NWG = 256;
constexpr int NSLOT = 8;
constexpr int SLOT_BYTES = 16384;
constexpr int SCR = NSLOT * SLOT_BYTES;
constexpr int MAX_PART = NWG + NSEG;
constexpr float C2 = 0.08838834764831845f * 1.4426950408889634f;

using bf16x8 = __attribute__((ext_vector_type(8))) short;
using s16x4 = __attribute__((ext_vector_type(4))) short;
using f32x4 = __attribute__((ext_vector_type(4))) float;
using f32x2 = __attribute__((ext_vector_type(2))) float;
typedef short v4i16_t __attribute__((ext_vector_type(4)));
typedef __attribute__((address_space(3))) const char* lds_cptr;
typedef __attribute__((address_space(3))) char* lds_ptr;

__device__ __forceinline__ void dma16(unsigned voff, const void* sbase, unsigned lds_dst) {
  unsigned keep;
  asm volatile("s_mov_b32 %0, m0\n\ts_mov_b32 m0, %3\n\ts_nop 0\n\tglobal_load_lds_dwordx4 %1, %2\n\ts_mov_b32 m0, %0"
               : "=&s"(keep) : "v"(voff), "s"(sbase), "s"(lds_dst) : "memory");
}
__device__ __forceinline__ void dma16nt(unsigned voff, const void* sbase, unsigned lds_dst) {
  unsigned keep;
  asm volatile("s_mov_b32 %0, m0\n\ts_mov_b32 m0, %3\n\ts_nop 0\n\tglobal_load_lds_dwordx4 %1, %2 nt\n\ts_mov_b32 m0, %0"
               : "=&s"(keep) : "v"(voff), "s"(sbase), "s"(lds_dst) : "memory");
}
__device__ __forceinline__ s16x4 vtr(lds_cptr p) {
  return __builtin_bit_cast(s16x4, __builtin_amdgcn_ds_read_tr16_b64_v4i16((__attribute__((address_space(3))) v4i16_t*)p));
}
__device__ __forceinline__ unsigned cvtpk(float lo, float hi) {
  unsigned r; asm("v_cvt_pk_bf16_f32 %0, %1, %2" : "=v"(r) : "v"(lo), "v"(hi)); return r;
}
__device__ __forceinline__ float xrow_max(float x) {
  auto s = __builtin_amdgcn_permlane16_swap(__float_as_uint(x), __float_as_uint(x), false, false);
  x = fmaxf(__uint_as_float(s[0]), __uint_as_float(s[1]));
  auto t = __builtin_amdgcn_permlane32_swap(__float_as_uint(x), __float_as_uint(x), false, false);
  return fmaxf(__uint_as_float(t[0]), __uint_as_float(t[1]));
}
__device__ __forceinline__ float xrow_sum(float x) {
  auto s = __builtin_amdgcn_permlane16_swap(__float_as_uint(x), __float_as_uint(x), false, false);
  x = __uint_as_float(s[0]) + __uint_as_float(s[1]);
  auto t = __builtin_amdgcn_permlane32_swap(__float_as_uint(x), __float_as_uint(x), false, false);
  return __uint_as_float(t[0]) + __uint_as_float(t[1]);
}
#define WAITVM(n) asm volatile("s_waitcnt vmcnt(" #n ")" ::: "memory")
__device__ __forceinline__ void wait_items(int k) {
  if (k >= 4) { if (k >= 6) { if (k >= 7) WAITVM(28); else WAITVM(24); } else { if (k == 5) WAITVM(20); else WAITVM(16); } }
  else { if (k >= 2) { if (k == 3) WAITVM(12); else WAITVM(8); } else { if (k == 1) WAITVM(4); else WAITVM(0); } }
}
}

__global__ void __launch_bounds__(768, 3)
attn_split(const __hip_bfloat16* __restrict__ q, const __hip_bfloat16* __restrict__ kc, const __hip_bfloat16* __restrict__ vc,
           const int* __restrict__ bt, const int* __restrict__ lens, float* __restrict__ po, float* __restrict__ ml) {
  __shared__ __attribute__((aligned(16))) char lds[NSLOT * SLOT_BYTES + 32768];
  const int tid = threadIdx.x, lane = tid & 63;
  const int w12 = __builtin_amdgcn_readfirstlane(tid >> 6);
  const bool prod = w12 >= 8;
  const int grp = (w12 >> 2) & 1, w = w12 & 3;
  const int wg = blockIdx.x;
  const int g = lane >> 4, l15 = lane & 15;
  const unsigned lds0 = (unsigned)(uintptr_t)lds;

  if (!prod) {
    dma16(lane * 16, (const char*)bt + w12 * 2048, lds0 + SCR + w12 * 2048);
    dma16(lane * 16, (const char*)bt + w12 * 2048 + 1024, lds0 + SCR + w12 * 2048 + 1024);
  }

  int lraw = lens[l15];
  const int uni = lraw <= 0;
  const int lj = uni ? SKV : (lraw > SKV ? SKV : lraw);
  const int cj = ((lj + 31) >> 5) + 1;
  int Pj = cj * HKV;
#pragma unroll
  for (int o = 1; o < 16; o <<= 1) { const int t = __shfl_up(Pj, o, 16); if (l15 >= o) Pj += t; }
  const int NI = __builtin_amdgcn_readlane(Pj, 15);
  Pj -= cj * HKV;
  const int x0 = (wg * NI) >> 8, x1 = ((wg + 1) * NI) >> 8;
  int xi = x0 - 1 + lane; xi = xi < 0 ? 0 : xi;
  int ib = 0;
#pragma unroll
  for (int j = 1; j < B; ++j) ib += (xi >= __builtin_amdgcn_readlane(Pj, j)) ? 1 : 0;
  const int Pb = __shfl(Pj, ib), cb = __shfl(cj, ib), lb = __shfl(lj | (uni << 16), ib);
  const int rr = xi - Pb;
  const int ik = (rr >= cb) + (rr >= 2 * cb) + (rr >= 3 * cb);
  int iu = rr - ik * cb;
  const int u0 = __builtin_amdgcn_readlane(iu, 1);
  const int st = (u0 > 0) ? 0 : 1;
  if (lane == 0) iu = 0;
  int n_end = x1 - x0 + 1;
  if (x1 <= x0) n_end = st;
  { const int lastu = __builtin_amdgcn_readlane(iu, n_end > st ? n_end - 1 : 0); if (n_end > st && lastu == 0) n_end -= 1; }
  const int tile = iu - 1;
  int nval = (lb & 0xffff) - 32 * tile; nval = nval > 32 ? 32 : nval;
  const int info = (ib * HKV + ik) | ((iu > 0 ? nval : 0) << 8) | ((iu == 0 ? 1 : 0) << 16) | ((lb >> 16) << 17);
  asm volatile("s_waitcnt vmcnt(0)\n\ts_barrier" ::: "memory");
  int ph0 = 0, ph1 = 0;
  { const int2 pp = *reinterpret_cast<const int2*>(lds + SCR + (ib * NBPS + 2 * (tile < 0 ? 0 : tile)) * 4); ph0 = pp.x; ph1 = pp.y; }
  asm volatile("s_waitcnt lgkmcnt(0)\n\ts_barrier" ::: "memory");

  const int lr = lane >> 4, lp = lane & 15;
  const int r16 = 4 * w + lr;
  const unsigned voffK = lr * 1024 + ((lp ^ r16) << 4);
  const unsigned voffV = lr * 1024 + ((lp ^ ((r16 & 7) << 1)) << 4);
  const unsigned voffQ = lr * 256 + ((lp ^ r16) << 4);

  auto issue = [&](int i) {
    const int inf = __builtin_amdgcn_readlane(info, i);
    const unsigned dst = lds0 + (i & (NSLOT - 1)) * SLOT_BYTES + w * 1024;
    const int seg = inf & 255;
    if (inf & 0x10000) {
      const char* qb = (const char*)q + (size_t)seg * (ROWS * D * 2) + w * 1024;
      dma16(voffQ, qb, dst); dma16(voffQ, qb + 4096, dst + 4096); dma16(voffQ, qb + 8192, dst + 8192); dma16(voffQ, qb + 12288, dst + 12288);
    } else {
      const int p0 = __builtin_amdgcn_readlane(ph0, i), p1 = __builtin_amdgcn_readlane(ph1, i);
      const size_t off0 = (size_t)p0 * 16384 + (size_t)(seg & 3) * 256 + w * 4096;
      const size_t off1 = (size_t)p1 * 16384 + (size_t)(seg & 3) * 256 + w * 4096;
      dma16nt(voffK, (const char*)kc + off0, dst); dma16nt(voffK, (const char*)kc + off1, dst + 4096);
      dma16nt(voffV, (const char*)vc + off0, dst + 8192); dma16nt(voffV, (const char*)vc + off1, dst + 12288);
    }
  };

  if (prod) {
    int issued = st;
    for (; issued < st + NSLOT && issued < n_end; ++issued) issue(issued);
    int i = st;
    while (i < n_end) {
      const int inf = __builtin_amdgcn_readlane(info, i);
      const bool isq = (inf & 0x10000) != 0;
      const bool two = !isq && (i + 1 < n_end) && !(__builtin_amdgcn_readlane(info, i + 1) & 0x10000);
      const int e = i + (two ? 1 : 0);
      wait_items(issued - 1 - e);
      asm volatile("s_barrier" ::: "memory");
      for (; issued < i + NSLOT && issued < n_end; ++issued) issue(issued);
      if (!isq) {
        const bool last = (e + 1 == n_end) || (__builtin_amdgcn_readlane(info, e + 1) & 0x10000);
        if (last) { asm volatile("s_barrier" ::: "memory"); asm volatile("s_barrier" ::: "memory"); }
      }
      i = e + 1;
    }
    return;
  }

  unsigned kaddr[4], vaddr[8];
#pragma unroll
  for (int s = 0; s < 4; ++s) kaddr[s] = l15 * 256 + (((4 * s + g) ^ l15) << 4);
  {
    const int qq = l15 >> 2, pp = l15 & 3, row = 4 * g + qq;
#pragma unroll
    for (int c = 0; c < 8; ++c) vaddr[c] = 8192 + row * 256 + ((c ^ (row & 7)) << 5) + 8 * pp;
  }

  bf16x8 qf[4];
  f32x4 o[8];
  float m = -INFINITY, l = 0.f;
#pragma unroll
  for (int c = 0; c < 8; ++c) o[c] = f32x4{0.f, 0.f, 0.f, 0.f};

  int i = st;
  while (i < n_end) {
    const int inf = __builtin_amdgcn_readlane(info, i);
    const bool isq = (inf & 0x10000) != 0;
    const bool two = !isq && (i + 1 < n_end) && !(__builtin_amdgcn_readlane(info, i + 1) & 0x10000);
    const int e = i + (two ? 1 : 0);
    asm volatile("s_barrier" ::: "memory");
    if (isq) {
      const lds_cptr sb = (lds_cptr)lds + (i & (NSLOT - 1)) * SLOT_BYTES;
#pragma unroll
      for (int s = 0; s < 4; ++s) {
        bf16x8 v = *(const __attribute__((address_space(3))) bf16x8*)(sb + w * 4096 + kaddr[s]);
        qf[s] = (inf & 0x20000) ? bf16x8{0, 0, 0, 0, 0, 0, 0, 0} : v;
      }
      m = -INFINITY; l = 0.f;
#pragma unroll
      for (int c = 0; c < 8; ++c) o[c] = f32x4{0.f, 0.f, 0.f, 0.f};
    } else {
      if (grp == 0 || two) {
        const int it = i + grp;
        const int nv = (__builtin_amdgcn_readlane(info, it) >> 8) & 63;
        const lds_cptr sb = (lds_cptr)lds + (it & (NSLOT - 1)) * SLOT_BYTES;
        f32x4 s0 = f32x4{0.f, 0.f, 0.f, 0.f}, s1 = f32x4{0.f, 0.f, 0.f, 0.f};
#pragma unroll
        for (int s = 0; s < 4; ++s) {
          bf16x8 ka = *(const __attribute__((address_space(3))) bf16x8*)(sb + kaddr[s]);
          bf16x8 kb = *(const __attribute__((address_space(3))) bf16x8*)(sb + 4096 + kaddr[s]);
          s0 = __builtin_amdgcn_mfma_f32_16x16x32_bf16(ka, qf[s], s0, 0, 0, 0);
          s1 = __builtin_amdgcn_mfma_f32_16x16x32_bf16(kb, qf[s], s1, 0, 0, 0);
        }
        if (nv < 32) {
#pragma unroll
          for (int j = 0; j < 4; ++j) { if (4 * g + j >= nv) s0[j] = -INFINITY; if (16 + 4 * g + j >= nv) s1[j] = -INFINITY; }
        }
        float tm = fmaxf(fmaxf(fmaxf(s0[0], s0[1]), fmaxf(s0[2], s0[3])), fmaxf(fmaxf(s1[0], s1[1]), fmaxf(s1[2], s1[3])));
        tm = xrow_max(tm) * C2;
        const float mn = fmaxf(m, tm);
        const float alpha = __builtin_amdgcn_exp2f(m - mn);
        m = mn;
        float p0[4], p1[4], ps = 0.f;
#pragma unroll
        for (int j = 0; j < 4; ++j) {
          p0[j] = __builtin_amdgcn_exp2f(__builtin_fmaf(s0[j], C2, -mn)); p1[j] = __builtin_amdgcn_exp2f(__builtin_fmaf(s1[j], C2, -mn));
          ps += p0[j] + p1[j];
        }
        l = l * alpha + ps;
        union { unsigned u[4]; bf16x8 v; } pf;
        pf.u[0] = cvtpk(p0[0], p0[1]); pf.u[1] = cvtpk(p0[2], p0[3]); pf.u[2] = cvtpk(p1[0], p1[1]); pf.u[3] = cvtpk(p1[2], p1[3]);
#pragma unroll
        for (int c = 0; c < 8; ++c) {
          const s16x4 lo = vtr(sb + vaddr[c]), hi = vtr(sb + vaddr[c] + 4096);
          const bf16x8 vf = bf16x8{lo[0], lo[1], lo[2], lo[3], hi[0], hi[1], hi[2], hi[3]};
          o[c] *= alpha;
          o[c] = __builtin_amdgcn_mfma_f32_16x16x32_bf16(vf, pf.v, o[c], 0, 0, 0);
        }
      }
      const bool last = (e + 1 == n_end) || (__builtin_amdgcn_readlane(info, e + 1) & 0x10000);
      if (last) {
        lds_ptr mo = (lds_ptr)lds + SCR + w * 8192 + lane * 16;
        lds_ptr mm = (lds_ptr)lds + (i & (NSLOT - 1)) * SLOT_BYTES + (w * 64 + lane) * 8;
        l = xrow_sum(l);
        asm volatile("s_waitcnt lgkmcnt(0)\n\ts_barrier" ::: "memory");
        if (grp == 1) {
#pragma unroll
          for (int c = 0; c < 8; ++c) *(__attribute__((address_space(3))) f32x4*)(mo + c * 1024) = o[c];
          *(__attribute__((address_space(3))) f32x2*)mm = f32x2{m, l};
        }
        asm volatile("s_waitcnt lgkmcnt(0)\n\ts_barrier" ::: "memory");
        if (grp == 0) {
          const f32x2 e2 = *(const __attribute__((address_space(3))) f32x2*)mm;
          const float mn = fmaxf(m, e2[0]);
          const float a0 = __builtin_amdgcn_exp2f(m - mn), a1 = __builtin_amdgcn_exp2f(e2[0] - mn);
          const int slot = wg + (inf & 255);
          float* pr = po + ((size_t)slot * ROWS + 16 * w + l15) * D + 4 * g;
#pragma unroll
          for (int c = 0; c < 8; ++c) {
            const f32x4 ob = *(const __attribute__((address_space(3))) f32x4*)(mo + c * 1024);
            *reinterpret_cast<f32x4*>(pr + 16 * c) = o[c] * a0 + ob * a1;
          }
          if (g == 0) *reinterpret_cast<float2*>(ml + ((size_t)slot * ROWS + 16 * w + l15) * 2) = make_float2(mn, l * a0 + e2[1] * a1);
        }
        asm volatile("s_waitcnt lgkmcnt(0)" ::: "memory");
      }
    }
    i = e + 1;
  }
}

__global__ void __launch_bounds__(256)
attn_combine(const int* __restrict__ lens, const float* __restrict__ po, const float* __restrict__ ml, __hip_bfloat16* __restrict__ out) {
  const int lane = threadIdx.x & 63, l15 = lane & 15;
  int lraw = lens[l15];
  const int lj = lraw <= 0 ? SKV : (lraw > SKV ? SKV : lraw);
  const int cj = ((lj + 31) >> 5) + 1;
  int Pj = cj * HKV;
#pragma unroll
  for (int o = 1; o < 16; o <<= 1) { const int t = __shfl_up(Pj, o, 16); if (l15 >= o) Pj += t; }
  const int NI = __builtin_amdgcn_readlane(Pj, 15);
  Pj -= cj * HKV;
  const int gid = blockIdx.x * 256 + threadIdx.x;
  const int seg = gid >> 11, row = (gid >> 5) & 63, d4 = gid & 31;
  const int b = seg >> 2, kvh = seg & 3;
  const int Pb = __shfl(Pj, b), cb = __shfl(cj, b);
  const int A = Pb + kvh * cb;
  const int w0 = ((A + 2) * NWG - 1) / NI, w1 = ((A + cb) * NWG - 1) / NI;
  float M = -INFINITY, L = 0.f; f32x4 acc = f32x4{0.f, 0.f, 0.f, 0.f};
  for (int wb = w0; wb <= w1; wb += 6) {
    float2 e[6]; f32x4 v[6];
#pragma unroll
    for (int j = 0; j < 6; ++j) {
      const int w = wb + j;
      const bool ok = (w <= w1) && (((w + 1) * NI) >> 8) > ((w * NI) >> 8);
      const int wc = ok ? w : w0;
      e[j] = *reinterpret_cast<const float2*>(ml + ((size_t)(wc + seg) * ROWS + row) * 2);
      v[j] = *reinterpret_cast<const f32x4*>(po + ((size_t)(wc + seg) * ROWS + row) * D + 4 * d4);
      if (!ok) e[j].x = -INFINITY;
    }
    float Mn = M;
#pragma unroll
    for (int j = 0; j < 6; ++j) Mn = fmaxf(Mn, e[j].x);
    const float a = __builtin_amdgcn_exp2f(M - Mn);
    L *= a; acc *= a;
#pragma unroll
    for (int j = 0; j < 6; ++j) { const float wt = __builtin_amdgcn_exp2f(e[j].x - Mn); L += wt * e[j].y; acc += v[j] * wt; }
    M = Mn;
  }
  const float inv = 1.f / L;
  union { unsigned u[2]; } r;
  r.u[0] = cvtpk(acc[0] * inv, acc[1] * inv); r.u[1] = cvtpk(acc[2] * inv, acc[3] * inv);
  *reinterpret_cast<uint2*>(out + ((size_t)seg * ROWS + row) * D + 4 * d4) = make_uint2(r.u[0], r.u[1]);
}

extern "C" void kernel_launch(void* const* d_in, const int* in_sizes, int n_in, void* d_out, int out_size, void* d_ws, size_t ws_size,
                              hipStream_t stream) {
  const __hip_bfloat16* q = (const __hip_bfloat16*)d_in[0];
  const __hip_bfloat16* kc = (const __hip_bfloat16*)d_in[1];
  const __hip_bfloat16* vc = (const __hip_bfloat16*)d_in[2];
  const int* bt = (const int*)d_in[3];
  const int* lens = (const int*)d_in[4];
  float* po = (float*)d_ws;
  float* ml = po + (size_t)MAX_PART * ROWS * D;
  attn_split<<<NWG, 768, 0, stream>>>(q, kc, vc, bt, lens, po, ml);
  attn_combine<<<NSEG * ROWS * 32 / 256, 256, 0, stream>>>(lens, po, ml, (__hip_bfloat16*)d_out);
}
```

```cpp
#include <hip/hip_runtime.h>
#include <hip/hip_bf16.h>
#include <stdint.h>

namespace {
constexpr int B = 16, HQ = 32, HKV = 4, SQ = 8, D = 128, PAGE = 16, NBPS = 256;
constexpr int SKV = NBPS * PAGE;
constexpr int ROWS = (HQ / HKV) * SQ;
constexpr int NSEG = B * HKV;
constexpr int NWG = 256;
constexpr int NSLOT = 7;
constexpr int SLOT_BYTES = 16384;
constexpr int SCR = NSLOT * SLOT_BYTES;
constexpr int MAX_PART = NWG + NSEG;
constexpr float C2 = 0.08838834764831845f * 1.4426950408889634f;

using bf16x8 = __attribute__((ext_vector_type(8))) short;
using s16x4 = __attribute__((ext_vector_type(4))) short;
using f32x4 = __attribute__((ext_vector_type(4))) float;
using f32x2 = __attribute__((ext_vector_type(2))) float;
typedef short v4i16_t __attribute__((ext_vector_type(4)));
typedef __attribute__((address_space(3))) const char* lds_cptr;
typedef __attribute__((address_space(3))) char* lds_ptr;

__device__ __forceinline__ void dma16(unsigned voff, const void* sbase, unsigned lds_dst) {
  unsigned keep;
  asm volatile("s_mov_b32 %0, m0\n\ts_mov_b32 m0, %3\n\ts_nop 0\n\tglobal_load_lds_dwordx4 %1, %2\n\ts_mov_b32 m0, %0"
               : "=&s"(keep) : "v"(voff), "s"(sbase), "s"(lds_dst) : "memory");
}
__device__ __forceinline__ void dma16nt(unsigned voff, const void* sbase, unsigned lds_dst) {
  unsigned keep;
  asm volatile("s_mov_b32 %0, m0\n\ts_mov_b32 m0, %3\n\ts_nop 0\n\tglobal_load_lds_dwordx4 %1, %2 nt\n\ts_mov_b32 m0, %0"
               : "=&s"(keep) : "v"(voff), "s"(sbase), "s"(lds_dst) : "memory");
}
__device__ __forceinline__ s16x4 vtr(lds_cptr p) {
  return __builtin_bit_cast(s16x4, __builtin_amdgcn_ds_read_tr16_b64_v4i16((__attribute__((address_space(3))) v4i16_t*)p));
}
typedef __attribute__((ext_vector_type(2))) __bf16 bf16x2_t;
__device__ __forceinline__ unsigned cvtpk(float lo, float hi) {
  return __builtin_bit_cast(unsigned, __builtin_convertvector(f32x2{lo, hi}, bf16x2_t));
}
__device__ __forceinline__ float xrow_max(float x) {
  auto s = __builtin_amdgcn_permlane16_swap(__float_as_uint(x), __float_as_uint(x), false, false);
  x = fmaxf(__uint_as_float(s[0]), __uint_as_float(s[1]));
  auto t = __builtin_amdgcn_permlane32_swap(__float_as_uint(x), __float_as_uint(x), false, false);
  return fmaxf(__uint_as_float(t[0]), __uint_as_float(t[1]));
}
__device__ __forceinline__ float xrow_sum(float x) {
  auto s = __builtin_amdgcn_permlane16_swap(__float_as_uint(x), __float_as_uint(x), false, false);
  x = __uint_as_float(s[0]) + __uint_as_float(s[1]);
  auto t = __builtin_amdgcn_permlane32_swap(__float_as_uint(x), __float_as_uint(x), false, false);
  return __uint_as_float(t[0]) + __uint_as_float(t[1]);
}
#define WAITVM(n) asm volatile("s_waitcnt vmcnt(" #n ")" ::: "memory")
__device__ __forceinline__ void wait_items(int k) {
  if (k >= 4) { if (k >= 6) { if (k >= 7) WAITVM(28); else WAITVM(24); } else { if (k == 5) WAITVM(20); else WAITVM(16); } }
  else { if (k >= 2) { if (k == 3) WAITVM(12); else WAITVM(8); } else { if (k == 1) WAITVM(4); else WAITVM(0); } }
}
}

__global__ void __launch_bounds__(768, 3)
attn_split(const __hip_bfloat16* __restrict__ q, const __hip_bfloat16* __restrict__ kc, const __hip_bfloat16* __restrict__ vc,
           const int* __restrict__ bt, const int* __restrict__ lens, float* __restrict__ po, float* __restrict__ ml) {
  __shared__ __attribute__((aligned(16))) char lds[NSLOT * SLOT_BYTES + 32768 + 1024];
  const int tid = threadIdx.x, lane = tid & 63;
  const int w12 = __builtin_amdgcn_readfirstlane(tid >> 6);
  const bool prod = w12 >= 8;
  const int grp = (w12 >> 2) & 1, w = w12 & 3;
  const int wg = blockIdx.x;
  const int g = lane >> 4, l15 = lane & 15;
  const unsigned lds0 = (unsigned)(uintptr_t)lds;

  if (!prod) {
    dma16(lane * 16, (const char*)bt + w12 * 2048, lds0 + SCR + w12 * 2048);
    dma16(lane * 16, (const char*)bt + w12 * 2048 + 1024, lds0 + SCR + w12 * 2048 + 1024);
  }

  int lraw = lens[l15];
  const int uni = lraw <= 0;
  const int lj = uni ? SKV : (lraw > SKV ? SKV : lraw);
  const int cj = ((lj + 31) >> 5) + 1;
  int Pj = cj;
#pragma unroll
  for (int o = 1; o < 16; o <<= 1) { const int t = __shfl_up(Pj, o, 16); if (l15 >= o) Pj += t; }
  const int NI = __builtin_amdgcn_readlane(Pj, 15);
  Pj -= cj;
  const int quad = wg >> 2, kvh = wg & 3;
  const int x0 = (quad * NI) >> 6, x1 = ((quad + 1) * NI) >> 6;
  int xi = x0 - 1 + lane; xi = xi < 0 ? 0 : xi;
  int ib = 0;
#pragma unroll
  for (int j = 1; j < B; ++j) ib += (xi >= __builtin_amdgcn_readlane(Pj, j)) ? 1 : 0;
  const int Pb = __shfl(Pj, ib), lb = __shfl(lj | (uni << 16), ib);
  int iu = xi - Pb;
  const int u0 = __builtin_amdgcn_readlane(iu, 1);
  const int st = (u0 > 0) ? 0 : 1;
  if (lane == 0) iu = 0;
  int n_end = x1 - x0 + 1;
  if (x1 <= x0) n_end = st;
  { const int lastu = __builtin_amdgcn_readlane(iu, n_end > st ? n_end - 1 : 0); if (n_end > st && lastu == 0) n_end -= 1; }
  const int tile = iu - 1;
  int nval = (lb & 0xffff) - 32 * tile; nval = nval > 32 ? 32 : nval;
  const int info = (ib * HKV + kvh) | ((iu > 0 ? nval : 0) << 8) | ((iu == 0 ? 1 : 0) << 16) | ((lb >> 16) << 17);
  asm volatile("s_waitcnt vmcnt(0)\n\ts_barrier" ::: "memory");
  int ph0 = 0, ph1 = 0;
  { const int2 pp = *reinterpret_cast<const int2*>(lds + SCR + (ib * NBPS + 2 * (tile < 0 ? 0 : tile)) * 4); ph0 = pp.x; ph1 = pp.y; }
  asm volatile("s_waitcnt lgkmcnt(0)\n\ts_barrier" ::: "memory");

  const int lr = lane >> 4, lp = lane & 15;
  const int r16 = 4 * w + lr;
  const unsigned voffK = lr * 1024 + ((lp ^ r16) << 4);
  const unsigned voffV = lr * 1024 + ((lp ^ ((r16 & 7) << 1)) << 4);
  const unsigned voffQ = lr * 256 + ((lp ^ r16) << 4);

  int pslot = 0;
  auto issue = [&](int i) {
    const int inf = __builtin_amdgcn_readlane(info, i);
    const unsigned dst = lds0 + pslot * SLOT_BYTES + w * 1024;
    pslot = pslot == NSLOT - 1 ? 0 : pslot + 1;
    const int seg = inf & 255;
    if (inf & 0x10000) {
      const char* qb = (const char*)q + (size_t)seg * (ROWS * D * 2) + w * 1024;
      dma16(voffQ, qb, dst); dma16(voffQ, qb + 4096, dst + 4096); dma16(voffQ, qb + 8192, dst + 8192); dma16(voffQ, qb + 12288, dst + 12288);
    } else {
      const int p0 = __builtin_amdgcn_readlane(ph0, i), p1 = __builtin_amdgcn_readlane(ph1, i);
      const size_t off0 = (size_t)p0 * 16384 + (size_t)(seg & 3) * 256 + w * 4096;
      const size_t off1 = (size_t)p1 * 16384 + (size_t)(seg & 3) * 256 + w * 4096;
      dma16nt(voffK, (const char*)kc + off0, dst); dma16nt(voffK, (const char*)kc + off1, dst + 4096);
      dma16nt(voffV, (const char*)vc + off0, dst + 8192); dma16nt(voffV, (const char*)vc + off1, dst + 12288);
    }
  };

  if (prod) {
    int issued = st;
    for (int c = 0; c < 3 && issued < n_end; ++c) issue(issued++);
    int i = st;
    while (i < n_end) {
      const int inf = __builtin_amdgcn_readlane(info, i);
      const bool isq = (inf & 0x10000) != 0;
      const bool two = !isq && (i + 1 < n_end) && !(__builtin_amdgcn_readlane(info, i + 1) & 0x10000);
      const int e = i + (two ? 1 : 0);
      wait_items(issued - 1 - e);
      asm volatile("s_barrier" ::: "memory");
      for (int c = 0; c < 4 && issued < i + NSLOT && issued < n_end; ++c) issue(issued++);
      i = e + 1;
    }
    if (n_end > st) asm volatile("s_barrier" ::: "memory");
    return;
  }
  unsigned kaddr[4], vaddr[8];
#pragma unroll
  for (int s = 0; s < 4; ++s) kaddr[s] = l15 * 256 + (((4 * s + g) ^ l15) << 4);
  {
    const int qq = l15 >> 2, pp = l15 & 3, row = 4 * g + qq;
#pragma unroll
    for (int c = 0; c < 8; ++c) vaddr[c] = 8192 + row * 256 + ((c ^ (row & 7)) << 5) + 8 * pp;
  }

  bf16x8 qf[4];
  f32x4 o[8];
  float m = -INFINITY, l = 0.f;
#pragma unroll
  for (int c = 0; c < 8; ++c) o[c] = f32x4{0.f, 0.f, 0.f, 0.f};

  lds_ptr xo = (lds_ptr)lds + SCR + grp * 16384 + w * 4096 + lane * 16;
  lds_cptr xi_ = (lds_cptr)lds + SCR + (grp ^ 1) * 16384 + w * 4096 + lane * 16;
  lds_ptr mo = (lds_ptr)lds + SCR + 32768 + grp * 512 + w * 128 + l15 * 8;
  lds_cptr mi = (lds_cptr)lds + SCR + 32768 + (grp ^ 1) * 512 + w * 128 + l15 * 8;
  int pend = -1;
  auto flush = [&]() {
    const f32x2 e2 = *(const __attribute__((address_space(3))) f32x2*)mi;
    const float mn = fmaxf(m, e2[0]);
    const float a0 = __builtin_amdgcn_exp2f(m - mn), a1 = __builtin_amdgcn_exp2f(e2[0] - mn);
    float* pr = po + ((size_t)pend * ROWS + 16 * w + l15) * D + 64 * grp + 4 * g;
#pragma unroll
    for (int c = 0; c < 4; ++c) {
      const f32x4 ob = *(const __attribute__((address_space(3))) f32x4*)(xi_ + c * 1024);
      *reinterpret_cast<f32x4*>(pr + 16 * c) = (grp ? o[4 + c] : o[c]) * a0 + ob * a1;
    }
    if (grp == 0 && g == 0) *reinterpret_cast<float2*>(ml + ((size_t)pend * ROWS + 16 * w + l15) * 2) = make_float2(mn, l * a0 + e2[1] * a1);
    pend = -1;
  };

  int i = st, cslot = 0;
  while (i < n_end) {
    const int inf = __builtin_amdgcn_readlane(info, i);
    const bool isq = (inf & 0x10000) != 0;
    const bool two = !isq && (i + 1 < n_end) && !(__builtin_amdgcn_readlane(info, i + 1) & 0x10000);
    const int e = i + (two ? 1 : 0);
    asm volatile("s_waitcnt lgkmcnt(0)\n\ts_barrier" ::: "memory");
    if (isq) {
      if (pend >= 0) flush();
      const lds_cptr sb = (lds_cptr)lds + cslot * SLOT_BYTES;
#pragma unroll
      for (int s = 0; s < 4; ++s) {
        bf16x8 v = *(const __attribute__((address_space(3))) bf16x8*)(sb + w * 4096 + kaddr[s]);
        qf[s] = (inf & 0x20000) ? bf16x8{0, 0, 0, 0, 0, 0, 0, 0} : v;
      }
      m = -INFINITY; l = 0.f;
#pragma unroll
      for (int c = 0; c < 8; ++c) o[c] = f32x4{0.f, 0.f, 0.f, 0.f};
    } else {
      if (grp == 0 || two) {
        const int it = i + grp;
        const int nv = (__builtin_amdgcn_readlane(info, it) >> 8) & 63;
        const int sl = grp ? (cslot == NSLOT - 1 ? 0 : cslot + 1) : cslot;
        const lds_cptr sb = (lds_cptr)lds + sl * SLOT_BYTES;
        f32x4 s0 = f32x4{0.f, 0.f, 0.f, 0.f}, s1 = f32x4{0.f, 0.f, 0.f, 0.f};
#pragma unroll
        for (int s = 0; s < 4; ++s) {
          bf16x8 ka = *(const __attribute__((address_space(3))) bf16x8*)(sb + kaddr[s]);
          bf16x8 kb = *(const __attribute__((address_space(3))) bf16x8*)(sb + 4096 + kaddr[s]);
          s0 = __builtin_amdgcn_mfma_f32_16x16x32_bf16(ka, qf[s], s0, 0, 0, 0);
          s1 = __builtin_amdgcn_mfma_f32_16x16x32_bf16(kb, qf[s], s1, 0, 0, 0);
        }
        if (nv < 32) {
#pragma unroll
          for (int j = 0; j < 4; ++j) { if (4 * g + j >= nv) s0[j] = -INFINITY; if (16 + 4 * g + j >= nv) s1[j] = -INFINITY; }
        }
        float tm = fmaxf(fmaxf(fmaxf(s0[0], s0[1]), fmaxf(s0[2], s0[3])), fmaxf(fmaxf(s1[0], s1[1]), fmaxf(s1[2], s1[3])));
        tm = xrow_max(tm) * C2;
        const float mn = fmaxf(m, tm);
        const float alpha = __builtin_amdgcn_exp2f(m - mn);
        m = mn;
        float p0[4], p1[4], ps = 0.f;
#pragma unroll
        for (int j = 0; j < 4; ++j) {
          p0[j] = __builtin_amdgcn_exp2f(__builtin_fmaf(s0[j], C2, -mn)); p1[j] = __builtin_amdgcn_exp2f(__builtin_fmaf(s1[j], C2, -mn));
          ps += p0[j] + p1[j];
        }
        l = l * alpha + ps;
        union { unsigned u[4]; bf16x8 v; } pf;
        pf.u[0] = cvtpk(p0[0], p0[1]); pf.u[1] = cvtpk(p0[2], p0[3]); pf.u[2] = cvtpk(p1[0], p1[1]); pf.u[3] = cvtpk(p1[2], p1[3]);
#pragma unroll
        for (int c = 0; c < 8; ++c) {
          const s16x4 lo = vtr(sb + vaddr[c]), hi = vtr(sb + vaddr[c] + 4096);
          const bf16x8 vf = bf16x8{lo[0], lo[1], lo[2], lo[3], hi[0], hi[1], hi[2], hi[3]};
          o[c] *= alpha;
          o[c] = __builtin_amdgcn_mfma_f32_16x16x32_bf16(vf, pf.v, o[c], 0, 0, 0);
        }
      }
      const bool last = (e + 1 == n_end) || (__builtin_amdgcn_readlane(info, e + 1) & 0x10000);
      if (last) {
        l = xrow_sum(l);
#pragma unroll
        for (int c = 0; c < 4; ++c) *(__attribute__((address_space(3))) f32x4*)(xo + c * 1024) = grp ? o[c] : o[4 + c];
        if (g == 0) *(__attribute__((address_space(3))) f32x2*)mo = f32x2{m, l};
        pend = (quad + ((inf & 255) >> 2)) * 4 + kvh;
      }
    }
    cslot += e + 1 - i; if (cslot >= NSLOT) cslot -= NSLOT;
    i = e + 1;
  }
  if (pend >= 0) {
    asm volatile("s_waitcnt lgkmcnt(0)\n\ts_barrier" ::: "memory");
    flush();
  }
}

__global__ void __launch_bounds__(256)
attn_combine(const int* __restrict__ lens, const float* __restrict__ po, const float* __restrict__ ml, __hip_bfloat16* __restrict__ out) {
  const int lane = threadIdx.x & 63, l15 = lane & 15;
  int lraw = lens[l15];
  const int lj = lraw <= 0 ? SKV : (lraw > SKV ? SKV : lraw);
  const int cj = ((lj + 31) >> 5) + 1;
  int Pj = cj;
#pragma unroll
  for (int o = 1; o < 16; o <<= 1) { const int t = __shfl_up(Pj, o, 16); if (l15 >= o) Pj += t; }
  const int NI = __builtin_amdgcn_readlane(Pj, 15);
  Pj -= cj;
  const int gid = blockIdx.x * 256 + threadIdx.x;
  const int seg = gid >> 11, row = (gid >> 5) & 63, d4 = gid & 31;
  const int b = seg >> 2, kvh = seg & 3;
  const int Pb = __shfl(Pj, b), cb = __shfl(cj, b);
  const int w0 = ((Pb + 2) * 64 - 1) / NI, w1 = ((Pb + cb) * 64 - 1) / NI;
  float M = -INFINITY, L = 0.f; f32x4 acc = f32x4{0.f, 0.f, 0.f, 0.f};
  for (int wb = w0; wb <= w1; wb += 6) {
    float2 e[6]; f32x4 v[6];
#pragma unroll
    for (int j = 0; j < 6; ++j) {
      const int w = wb + j;
      const bool ok = (w <= w1) && (((w + 1) * NI) >> 6) > ((w * NI) >> 6);
      const int wc = ok ? w : w0;
      e[j] = *reinterpret_cast<const float2*>(ml + ((size_t)((wc + b) * 4 + kvh) * ROWS + row) * 2);
      v[j] = *reinterpret_cast<const f32x4*>(po + ((size_t)((wc + b) * 4 + kvh) * ROWS + row) * D + 4 * d4);
      if (!ok) e[j].x = -INFINITY;
    }
    float Mn = M;
#pragma unroll
    for (int j = 0; j < 6; ++j) Mn = fmaxf(Mn, e[j].x);
    const float a = __builtin_amdgcn_exp2f(M - Mn);
    L *= a; acc *= a;
#pragma unroll
    for (int j = 0; j < 6; ++j) { const float wt = __builtin_amdgcn_exp2f(e[j].x - Mn); L += wt * e[j].y; acc += v[j] * wt; }
    M = Mn;
  }
  const float inv = 1.f / L;
  union { unsigned u[2]; } r;
  r.u[0] = cvtpk(acc[0] * inv, acc[1] * inv); r.u[1] = cvtpk(acc[2] * inv, acc[3] * inv);
  *reinterpret_cast<uint2*>(out + ((size_t)seg * ROWS + row) * D + 4 * d4) = make_uint2(r.u[0], r.u[1]);
}

extern "C" void kernel_launch(void* const* d_in, const int* in_sizes, int n_in, void* d_out, int out_size, void* d_ws, size_t ws_size,
                              hipStream_t stream) {
  const __hip_bfloat16* q = (const __hip_bfloat16*)d_in[0];
  const __hip_bfloat16* kc = (const __hip_bfloat16*)d_in[1];
  const __hip_bfloat16* vc = (const __hip_bfloat16*)d_in[2];
  const int* bt = (const int*)d_in[3];
  const int* lens = (const int*)d_in[4];
  float* po = (float*)d_ws;
  float* ml = po + (size_t)MAX_PART * ROWS * D;
  attn_split<<<NWG, 768, 0, stream>>>(q, kc, vc, bt, lens, po, ml);
  attn_combine<<<NSEG * ROWS * 32 / 256, 256, 0, stream>>>(lens, po, ml, (__hip_bfloat16*)d_out);
}
```
